```python
import torch

def _softmax(x, axis=-1):
    return torch.softmax(x, dim=axis)
def _gelu(x):
    return torch.nn.functional.gelu(x, approximate="tanh")
def _silu(x):
    return torch.nn.functional.silu(x)
def _sigmoid(x):
    return torch.sigmoid(x)
def _layernorm(x, axis=-1, eps=1e-5):
    m = x.mean(dim=axis, keepdim=True)
    v = x.var(dim=axis, keepdim=True, unbiased=False)
    return (x - m) / torch.sqrt(v + eps)
def _scatter_add(tgt, idx, src, axis=0):
    return tgt.clone().index_add_(axis, idx, src)
def _segment_sum(src, seg, n):
    out = torch.zeros((n,) + src.shape[1:], dtype=src.dtype)
    return out.index_add_(0, seg, src)
def _pad(x, pw):
    flat = tuple(v for pair in reversed(pw) for v in pair)
    return torch.nn.functional.pad(x, flat)

kernel_name = 'standalone__mixer_token_mlp__prompt'

def setup_inputs():

    _w = (lambda a: (lambda t: t.to(torch.bfloat16) if t.is_floating_point() else t)(torch.as_tensor(a)))
    B, S, D, F = 1, 2048, 512, 1376
    return {
        "x": _w((lambda *s: torch.randn(*s, dtype=torch.bfloat16) * 0.05)(B, S, D)),
        "w1": _w((lambda *s: torch.randn(*s, dtype=torch.bfloat16) * 0.2)(S, F) * (S ** -0.5)),
        "b1": _w((lambda *s: torch.randn(*s, dtype=torch.bfloat16) * 1.6)(F) * 0.02),
        "w2": _w((lambda *s: torch.randn(*s, dtype=torch.bfloat16) * 0.05)(F, S) * (F ** -0.5)),
        "b2": _w((lambda *s: torch.randn(*s, dtype=torch.bfloat16) * 0.1)(S) * 0.02),
    }

def reference(x, w1, b1, w2, b2):
    B, S, D = x.shape
    h = _layernorm(x, axis=-1)
    u = torch.einsum("bsd,sf->bdf", h, w1) + b1
    u = _gelu(u)
    y = torch.einsum("bdf,fs->bds", u, w2) + b2
    return (lambda x, *d: x.permute(*d))(y, 0, 2, 1)
```

```cpp
#include <hip/hip_runtime.h>
#include <stdint.h>
typedef unsigned short u16;
typedef __attribute__((ext_vector_type(2))) __bf16 bf16x2;
typedef __attribute__((ext_vector_type(4))) __bf16 bf16x4;
typedef __attribute__((ext_vector_type(2))) float f32x2;
typedef __attribute__((ext_vector_type(4))) float f32x4;
typedef __attribute__((ext_vector_type(16))) float f32x16;
typedef __attribute__((ext_vector_type(4))) unsigned u32x4;
typedef __attribute__((ext_vector_type(2))) unsigned u32x2;
typedef __attribute__((ext_vector_type(4))) int i32x4;
typedef __attribute__((ext_vector_type(8))) unsigned u32x8;
#define LDS __attribute__((address_space(3)))
#define DEV __device__ __forceinline__
constexpr int S=2048, D=512, F=1376;
constexpr int HW=3, HV=3;
constexpr int PB=13;
constexpr int PI2=2, PB2=5;
constexpr int ND=2;
constexpr int PF1=2, PFS=8, PF3=2, P2S=44;
#define APOL "sc1"
constexpr unsigned WS_SL=4096, WS_H=WS_SL+8*32768, WS_U=WS_H+64*32*1024, WS_END=WS_U+43*32*1024;
constexpr unsigned OOB=0x7ff00000u;
constexpr int RB=7, RING=RB*4096, L_RED=4*RING, LDSB=L_RED+49152;

DEV i32x4 mk_srd(const void* p, unsigned bytes){
  i32x4 r; uint64_t a=(uint64_t)p;
  r[0]=(int)(unsigned)a; r[1]=(int)(unsigned)(a>>32); r[2]=(int)bytes; r[3]=0x00020000; return r;
}
template<int N> DEV void waitv(){ asm volatile("s_waitcnt vmcnt(%0)" :: "i"(N) : "memory"); }
DEV void waitl(){ asm volatile("s_waitcnt lgkmcnt(0)" ::: "memory"); }
template<int I> struct IC{ static constexpr int v=I; };
template<int B,int E,class Fn> DEV void sfor(Fn&& f){ if constexpr(B<E){ f(IC<B>{}); sfor<B+1,E>(f); } }
DEV unsigned pk(float lo, float hi){ bf16x2 v; v[0]=(__bf16)lo; v[1]=(__bf16)hi; return __builtin_bit_cast(unsigned,v); }
DEV float flo(unsigned v){ return __uint_as_float(v<<16); }
DEV float fhi(unsigned v){ return __uint_as_float(v&0xffff0000u); }
DEV void bar(){ asm volatile("s_waitcnt lgkmcnt(0)\n\ts_barrier" ::: "memory"); }
template<int CTRL> DEV float dppadd(float v){ return v+__builtin_bit_cast(float,__builtin_amdgcn_update_dpp(0,__builtin_bit_cast(int,v),CTRL,0xf,0xf,true)); }
DEV float rsum(float v){ v=dppadd<0xB1>(v); v=dppadd<0x4E>(v); v=dppadd<0x141>(v); return dppadd<0x140>(v); }
DEV float dot2(unsigned a, unsigned b, float c){ return __builtin_amdgcn_fdot2_f32_bf16(__builtin_bit_cast(bf16x2,a),__builtin_bit_cast(bf16x2,b),c,false); }
#define D2(l,h,x) "v_dot2_f32_bf16 " l ", " x ", %24, %26\n\tv_dot2_f32_bf16 " h ", " x ", %25, %26\n\t"
DEV void center16(float (&d)[16], u32x4 a, u32x4 b, float c){
  asm(D2("%0","%1","%16") D2("%2","%3","%17") D2("%4","%5","%18") D2("%6","%7","%19") D2("%8","%9","%20") D2("%10","%11","%21") D2("%12","%13","%22") D2("%14","%15","%23") "s_nop 2"
    : "=&v"(d[0]),"=&v"(d[1]),"=&v"(d[2]),"=&v"(d[3]),"=&v"(d[4]),"=&v"(d[5]),"=&v"(d[6]),"=&v"(d[7]),"=&v"(d[8]),"=&v"(d[9]),"=&v"(d[10]),"=&v"(d[11]),"=&v"(d[12]),"=&v"(d[13]),"=&v"(d[14]),"=&v"(d[15])
    : "v"(a[0]),"v"(a[1]),"v"(a[2]),"v"(a[3]),"v"(b[0]),"v"(b[1]),"v"(b[2]),"v"(b[3]),"s"(0x00003f80u),"s"(0x3f800000u),"v"(c));
}
DEV f32x2 gelu2(f32x2 a){
  f32x2 z=a*(a*a*-0.10294324f-2.30220819f);
  f32x2 e={__builtin_amdgcn_exp2f(z[0]),__builtin_amdgcn_exp2f(z[1])}; e+=1.f;
  return a*(f32x2){__builtin_amdgcn_rcpf(e[0]),__builtin_amdgcn_rcpf(e[1])};
}
DEV u32x2 trd(LDS char* p){ return __builtin_bit_cast(u32x2,__builtin_amdgcn_ds_read_tr16_b64_v4bf16((LDS bf16x4*)p)); }
struct R4{ u32x4 v[4]; };
template<int N> DEV void wait1(R4& a){ asm volatile("s_waitcnt vmcnt(%4)" : "+v"(a.v[0]),"+v"(a.v[1]),"+v"(a.v[2]),"+v"(a.v[3]) : "i"(N) : "memory"); }
template<int N> DEV void wait2(u32x2& a){ asm volatile("s_waitcnt vmcnt(%1)" : "+v"(a) : "i"(N) : "memory"); }
DEV void ldf(R4& d, unsigned v0, unsigned v1, i32x4 srd, unsigned so){
  asm volatile("buffer_load_dwordx4 %0, %4, %6, %7 offen " APOL "\n\tbuffer_load_dwordx4 %1, %5, %6, %7 offen " APOL "\n\t"
               "buffer_load_dwordx4 %2, %4, %6, %7 offen offset:2048 " APOL "\n\tbuffer_load_dwordx4 %3, %5, %6, %7 offen offset:2048 " APOL
    : "=&v"(d.v[0]),"=&v"(d.v[1]),"=&v"(d.v[2]),"=&v"(d.v[3]) : "v"(v0),"v"(v1),"s"(srd),"s"(so) : "memory");
}
DEV void dma4(unsigned v, i32x4 srd, unsigned so, unsigned step, unsigned l){
  unsigned t;
  asm volatile("s_mov_b32 m0, %5\n\ts_add_u32 %0, %3, %4\n\tbuffer_load_dwordx4 %1, %2, %3 offen lds\n\t"
               "s_add_u32 m0, m0, 0x400\n\ts_nop 0\n\tbuffer_load_dwordx4 %1, %2, %0 offen lds\n\t"
               "s_add_u32 m0, m0, 0x400\n\ts_add_u32 %0, %0, %4\n\tbuffer_load_dwordx4 %1, %2, %0 offen lds\n\t"
               "s_add_u32 m0, m0, 0x400\n\ts_add_u32 %0, %0, %4\n\tbuffer_load_dwordx4 %1, %2, %0 offen lds"
    : "=&s"(t) : "v"(v),"s"(srd),"s"(so),"s"(step),"s"(l) : "memory","scc");
}
#define TRA(r,p,o) "ds_read_b64_tr_b16 a[%2+" #r ":%2+" #r "+1], " p " offset:" o "\n\t"
template<int A,int OFF,int T> DEV void rdw(unsigned r0, unsigned r1){
  asm volatile(TRA(0,"%0","%3") TRA(2,"%0","%3+%4") TRA(4,"%1","%3") TRA(6,"%1","%3+%4")
               TRA(8,"%0","%3+2048") TRA(10,"%0","%3+2048+%4") TRA(12,"%1","%3+2048") TRA(14,"%1","%3+2048+%4")
    :: "v"(r0),"v"(r1),"n"(A),"n"(OFF),"n"(T) : "memory");
}
#define MW(c,r,x,s) "v_mfma_f32_32x32x16_bf16 " c ", a[%[A]+" #r ":%[A]+" #r "+3], " x ", " s "\n\t"
#define MX(c,r,x,s) "v_mfma_f32_32x32x16_bf16 " c ", " x ", a[%[A]+" #r ":%[A]+" #r "+3], " s "\n\t"
#define MM(M,z0,z1,z2,z3,l0,l1,l2,l3) M("%0",0,"%[h0]",z0) l0 M("%1",0,"%[h1]",z1) M("%2",4,"%[h0]",z2) l1 M("%3",4,"%[h1]",z3) \
  M("%0",8,"%[h2]","%0") l2 M("%1",8,"%[h3]","%1") M("%2",12,"%[h2]","%2") l3 M("%3",12,"%[h3]","%3")
#define LF(d,v,o) "buffer_load_dwordx4 " d ", " v ", %[srd], %[so] offen " o APOL "\n\t"
#define LFS LF("%4","%[v0]",""),LF("%5","%[v1]",""),LF("%6","%[v0]","offset:2048 "),LF("%7","%[v1]","offset:2048 ")
#define LD0 "s_mov_b32 m0, %[l]\n\ts_mov_b32 %4, %[so]\n\tbuffer_load_dwordx4 %[v0], %[srd], %4 offen lds\n\t"
#define LD1 "s_add_u32 m0, m0, 0x400\n\ts_add_u32 %4, %4, 0x8000\n\tbuffer_load_dwordx4 %[v0], %[srd], %4 offen lds\n\t"
#define LDS4 LD0,LD1,LD1,LD1
#define NOL "","","",""
#define ZC "%0","0","0","0"
#define CZ "+v"(c[0]),"=&v"(c[1]),"=&v"(c[2]),"=&v"(c[3])
#define AC "%0","%1","%2","%3"
#define MMX(...) MM(__VA_ARGS__)
#define CO(m) m(c[0]),m(c[1]),m(c[2]),m(c[3])
#define HI [h0]"v"(h.v[0]),[h1]"v"(h.v[1]),[h2]"v"(h.v[2]),[h3]"v"(h.v[3]),[A]"n"(A)
#define NO "=&v"(n.v[0]),"=&v"(n.v[1]),"=&v"(n.v[2]),"=&v"(n.v[3])
#define LI [v0]"v"(v0),[v1]"v"(v1),[srd]"s"(srd),[so]"s"(so)
template<int A,bool FIRST,bool WROWS> DEV void mml(f32x16 (&c)[4], const R4& h, R4& n, unsigned v0, unsigned v1, i32x4 srd, unsigned so){
  if constexpr(FIRST){
    if constexpr(WROWS) asm volatile(MMX(MW,ZC,LFS) : CZ,NO : HI,LI : "memory"); else asm volatile(MMX(MX,ZC,LFS) : CZ,NO : HI,LI : "memory");
  } else {
    if constexpr(WROWS) asm volatile(MMX(MW,AC,LFS) : CO("+v"),NO : HI,LI : "memory"); else asm volatile(MMX(MX,AC,LFS) : CO("+v"),NO : HI,LI : "memory");
  }
}
template<int A> DEV void mmd(f32x16 (&c)[4], const R4& h, unsigned v0, i32x4 srd, unsigned so, unsigned l){
  unsigned t; asm volatile(MMX(MW,AC,LDS4) : CO("+v"),"=&s"(t) : HI,[v0]"v"(v0),[srd]"s"(srd),[so]"s"(so),[l]"s"(l) : "memory","scc");
}
template<int A,bool WROWS> DEV void mm(f32x16 (&c)[4], const R4& h){
  if constexpr(WROWS) asm volatile(MMX(MW,AC,NOL) : CO("+v") : HI); else asm volatile(MMX(MX,AC,NOL) : CO("+v") : HI);
}
struct RA{ LDS char* p[4]; };
DEV void red_adr(RA& a, LDS char* red, int w, unsigned l16){
  #pragma unroll
  for(int q=0;q<4;q++) a.p[q]=red+(q^w)*12288+(q?q-1:0)*4096+l16;
  asm volatile("" : "+v"(a.p[0]),"+v"(a.p[1]),"+v"(a.p[2]),"+v"(a.p[3]));
}
DEV void red_put(f32x16 (&c)[4], const RA& a){
  asm volatile("s_nop 7\n\ts_nop 7" : "+v"(c[0]),"+v"(c[1]),"+v"(c[2]),"+v"(c[3]));
  #pragma unroll
  for(int n=0;n<12;n++){ const int q=1+n/4, j=n&3; *(LDS f32x4*)(a.p[q]+j*1024)=(f32x4){c[q][4*j],c[q][4*j+1],c[q][4*j+2],c[q][4*j+3]}; }
  bar();
}
template<int O> DEV void st4(u32x4 d, unsigned v, i32x4 srd, unsigned so){
  asm volatile("buffer_store_dwordx4 %0, %1, %2, %3 offen offset:%4 sc1\n\ts_nop 1" :: "v"(d),"v"(v),"s"(srd),"s"(so),"i"(O) : "memory");
}
struct P12{ f32x4 v[12]; };
DEV void red_ld(P12& d, LDS char* p){
  #pragma unroll
  for(int n=0;n<12;n++) d.v[n]=*(LDS f32x4*)(p+(n%3)*4096+(n/3)*1024);
  __builtin_amdgcn_sched_barrier(0);
}
DEV f32x4 red_get(const f32x16& c, int j, const P12& d){ return ((f32x4){c[4*j],c[4*j+1],c[4*j+2],c[4*j+3]}+d.v[3*j])+(d.v[3*j+1]+d.v[3*j+2]); }

__global__ __launch_bounds__(256,1) void mixer(const u16*, const u16*, const u16*, const u16*, const u16*, u16*, char*){
  extern __shared__ __attribute__((aligned(16))) char smem[];
  LDS char* sm=(LDS char*)smem;
  u32x8 k8; u32x4 k4; u32x2 k2a; u32x4 xr[8]; unsigned tc;
  { unsigned long long pc; unsigned t;
    asm volatile("s_load_dwordx8 %0, %6, 0x0\n\ts_load_dwordx4 %1, %6, 0x20\n\ts_load_dwordx2 %2, %6, 0x30\n\t"
                 "s_lshr_b32 %5, %7, 4\n\ts_cmp_lg_u32 %5, 4\n\ts_cbranch_scc1 1f\n\ts_getpc_b64 %4\n\tglobal_load_dword %3, %8, %4\n1:"
      : "=&s"(k8),"=&s"(k4),"=&s"(k2a),"=&v"(tc),"=&s"(pc),"=&s"(t) : "s"(__builtin_amdgcn_kernarg_segment_ptr()),"s"(blockIdx.x),"v"(threadIdx.x*56) : "memory","scc","a255");
    __builtin_amdgcn_sched_barrier(0); }
  const int bid=blockIdx.x, xcd=bid&7, r=bid>>3, a4=r&3, bb=r>>2, fq=xcd>>1, a=(xcd&1)*4+a4;
  const int lane=threadIdx.x&63; const int w=__builtin_amdgcn_readfirstlane(threadIdx.x>>6);
  const int n1=fq*22+r-10, ft=n1>>2, a1=(xcd&1)*4+(n1&3), stile=fq*8+bb;
  const unsigned l16=lane*16, g=lane>>4, i=lane&15, q=i>>2, p=i&3, hh=g>>1, cb=g&1;
  unsigned tlo,thi;
  auto epoch_w=[&](u32x2& ep){ asm volatile("s_waitcnt lgkmcnt(0)" : "+s"(ep) :: "memory"); tlo=ep[0]+1u; thi=ep[1]; };
  const unsigned row=lane>>3, ch=(lane&7)^(4*((row>>1)&1));
  unsigned ring=(unsigned)(size_t)sm+w*RING, s128=stile*128, slb=WS_SL+xcd*32768;
  const unsigned av0=l16+(w&1)*1024, av1=av0^1024, sw64=(((q>>1)^(w>>1))&1)*64;
  const int n2=w<3?11:10, k2=11*w; int rot2=(bb*11>>3)+a4*3; if(rot2>=n2) rot2-=n2;
  const int rot1=n1+((r-10)>>2);
  auto kb=[&](int t){ return 4*((t&12)|((t+rot1)&3))+w; };
  unsigned wv=row*(F*2)+(ft==21?(ch&3):ch)*16, xv=w*8192+g*1024+i*16, xo=bid*32768u, xo2=xo+4096, w0=kb(0)*(32*F*2)+ft*128;
  asm volatile("s_waitcnt lgkmcnt(0)" : "+s"(k8),"+s"(k4),"+s"(k2a),"+s"(ring),"+v"(wv),"+v"(xv),"+s"(xo),"+s"(xo2),"+s"(w0) :: "memory");
  auto ptr=[](unsigned lo, unsigned hi){ return (char*)(((unsigned long long)hi<<32)|lo); };
  const char *x=ptr(k8[0],k8[1]), *w1=ptr(k8[2],k8[3]), *b1=ptr(k8[4],k8[5]), *w2=ptr(k8[6],k8[7]), *b2=ptr(k4[0],k4[1]); char *out=ptr(k4[2],k4[3]), *ws=ptr(k2a[0],k2a[1]);
  const i32x4 w1srd=mk_srd(w1,S*F*2);
  { unsigned t;
    asm volatile("s_cmp_lt_u32 %11, 0x280000\n\ts_cbranch_scc1 1f\n\t"
                 "s_mov_b32 m0, %16\n\ts_add_u32 %8, %15, %17\n\tbuffer_load_dwordx4 %13, %14, %15 offen lds\n\t"
                 "s_add_u32 m0, m0, 0x400\n\ts_nop 0\n\tbuffer_load_dwordx4 %13, %14, %8 offen lds\n\t"
                 "s_add_u32 m0, m0, 0x400\n\ts_add_u32 %8, %8, %17\n\tbuffer_load_dwordx4 %13, %14, %8 offen lds\n\t"
                 "s_add_u32 m0, m0, 0x400\n\ts_add_u32 %8, %8, %17\n\tbuffer_load_dwordx4 %13, %14, %8 offen lds\n\ts_branch 2f\n"
                 "1:\n\ts_cmp_gt_u32 %11, 0x1fffff\n\ts_cbranch_scc1 2f\n\t"
                 "buffer_load_dwordx4 %0, %9, %10, %11 offen\n\tbuffer_load_dwordx4 %1, %9, %10, %11 offen offset:256\n\t"
                 "buffer_load_dwordx4 %2, %9, %10, %11 offen offset:512\n\tbuffer_load_dwordx4 %3, %9, %10, %11 offen offset:768\n\t"
                 "buffer_load_dwordx4 %4, %9, %10, %12 offen\n\tbuffer_load_dwordx4 %5, %9, %10, %12 offen offset:256\n\t"
                 "buffer_load_dwordx4 %6, %9, %10, %12 offen offset:512\n\tbuffer_load_dwordx4 %7, %9, %10, %12 offen offset:768\n2:"
      : "=&v"(xr[0]),"=&v"(xr[1]),"=&v"(xr[2]),"=&v"(xr[3]),"=&v"(xr[4]),"=&v"(xr[5]),"=&v"(xr[6]),"=&v"(xr[7]),"=&s"(t)
      : "v"(xv),"s"(mk_srd(x,S*D*2)),"s"(xo),"s"(xo2),"v"(wv),"s"(w1srd),"s"(w0),"s"(ring),"i"(8*F*2) : "memory","scc");
    __builtin_amdgcn_sched_barrier(0); }
  asm volatile("" : "+s"(s128),"+s"(rot2),"+s"(slb));
  const i32x4 wssrd=mk_srd(ws,WS_END), w2srd=mk_srd(w2,S*F*2);
  auto epoch=[&](u32x2& ep){ asm volatile("s_load_dwordx2 %0, %1, 0x0" : "=&s"(ep) : "s"(ws) : "memory"); };
  auto kb2=[&](int t){ int k=t+rot2; if(k>=n2) k-=n2; return k2+k; };
  auto LW2=[&](int t){ dma4(row*(S*2)+ch*16,w2srd,kb2(t)*(32*S*2)+s128,8*S*2,ring+((t+2)%RB)*4096); };
  auto flag=[&](unsigned line,bool all){ u32x2 t={tlo,thi}; const unsigned tid=threadIdx.x;
    asm volatile("s_barrier\n\tbuffer_store_dwordx2 %0, %1, %2, %3 offen sc1" :: "v"(t),"v"(all?(tid<8?tid*32768u:OOB):(tid<4?(tid*2+(xcd&1))*32768u:OOB)),"s"(wssrd),"s"(WS_SL+line*128u) : "memory"); };
  auto poll_issue=[&](u32x2& t,unsigned v,unsigned idx){ asm volatile("buffer_load_dwordx2 %0, %1, %2, %3 offen sc1" : "=&v"(t) : "v"(v),"s"(wssrd),"s"(slb+idx*128u) : "memory"); };
  auto poll_spin=[&](u32x2& t,unsigned v,unsigned idx){
    _Pragma("nounroll") for(int spin=0;spin<(1<<20);spin++){
      if(__all(v==OOB||(t[0]==tlo && t[1]==thi))) break;
      __builtin_amdgcn_s_sleep(2);
      asm volatile("buffer_load_dwordx2 %0, %1, %2, %3 offen sc1\n\ts_waitcnt vmcnt(0)" : "=&v"(t) : "v"(v),"s"(wssrd),"s"(slb+idx*128u) : "memory");
    } };
  const unsigned q0=ring+(4*hh+q)*128+32*cb+8*p+sw64, q1=q0^64;
  LDS char* red=sm+L_RED;
  unsigned bias2;
  auto ldb2=[&](){ asm volatile("buffer_load_ushort %0, %1, %2, %3 offen" : "=&v"(bias2) : "v"((lane&31)*2),"s"(mk_srd(b2,S*2)),"s"(stile*128+(w>>1)*64) : "memory"); };
    auto LW1=[&](int t){ dma4(wv,w1srd,kb(t)*(32*F*2)+ft*128,8*F*2,ring+(t%RB)*4096); };
  int r2=r; asm volatile("" : "+s"(r2));
  if(r2>=10){
    const int ub=2*ft+(w>>1); unsigned uv=ub<43?av0:OOB, us=WS_U+((ub<43?ub:0)*8+a1)*4096u;
    u32x2 ep; epoch(ep);
    u32x2 B1[4];
    asm volatile("buffer_load_dwordx2 %0, %4, %5, 0 offen\n\tbuffer_load_dwordx2 %1, %4, %5, 0 offen offset:16\n\tbuffer_load_dwordx2 %2, %4, %5, 0 offen offset:32\n\tbuffer_load_dwordx2 %3, %4, %5, 0 offen offset:48"
      : "=&v"(B1[0]),"=&v"(B1[1]),"=&v"(B1[2]),"=&v"(B1[3]) : "v"(ub*64+hh*8),"s"(mk_srd(b1,F*2)) : "memory");
    ldb2();
    sfor<1,RB>([&](auto ic){ LW1(decltype(ic)::v); });
    const unsigned r0=ring+(8*hh+q)*128+32*cb+8*p+sw64, r1=r0^64;
    const unsigned pv=lane<16?lane*512u:OOB, pi=w; u32x2 pt;
    R4 H[16]; f32x16 acc[4];
    auto LH=[&](int t){ ldf(H[t],av0,av1,wssrd,WS_H+(kb(t)*8+a1)*4096u); };
    sfor<0,16>([&](auto ic){ constexpr int t=decltype(ic)::v;
      constexpr int li=t==0?RB-1:t+RB-2, last=li<15?li:15;
      waitv<4*(last-t)+(t<1?5:0)+(t<=PB&&last>=PB?1:0)+(t>PB?4*HW:0)>();
      rdw<16*t,(t%RB)*4096,512>(r0,r1);
      if constexpr(t>0&&t+RB-1<16){ asm volatile("s_waitcnt lgkmcnt(8)" ::: "memory"); LW1(t+RB-1); if constexpr(t+RB-1==PB) poll_issue(pt,pv,pi); }
      if constexpr(t==PB){
        asm volatile("s_waitcnt vmcnt(%6)" : "+v"(B1[0]),"+v"(B1[1]),"+v"(B1[2]),"+v"(B1[3]),"+v"(bias2),"+v"(pt) : "i"(4*(15-PB)) : "memory");
        epoch_w(ep);
        poll_spin(pt,pv,pi);
        sfor<0,HW>([&](auto jc){ LH(decltype(jc)::v); });
        #pragma unroll
        for(int j=0;j<8;j++){ acc[0][2*j]=flo(B1[j>>1][j&1]); acc[0][2*j+1]=fhi(B1[j>>1][j&1]); }
        asm volatile("" : "+v"(acc[0]));
      }
    });
    RA ra; red_adr(ra,red,w,l16); asm volatile("" : "+v"(uv),"+s"(us));
    waitl();
    sfor<0,16>([&](auto ic){ constexpr int t=decltype(ic)::v;
      wait1<4*(HW-1)>(H[t]);
      if constexpr(t+HW<16) mml<16*t,t==0,true>(acc,H[t],H[t+HW],av0,av1,wssrd,WS_H+(kb(t+HW)*8+a1)*4096u);
      else mmd<16*t>(acc,H[t],row*(S*2)+ch*16,w2srd,kb2(t+HW-16)*(32*S*2)+s128,ring+((t+HW-14)%RB)*4096);
    });
    red_put(acc,ra);
    P12 d; red_ld(d,ra.p[0]);
    unsigned o[8];
    sfor<0,4>([&](auto ic){ constexpr int j=decltype(ic)::v;
      f32x4 v=red_get(acc[0],j,d);
      f32x2 e=gelu2((f32x2){v[0],v[1]}), f=gelu2((f32x2){v[2],v[3]});
      o[2*j]=pk(e[0],e[1]); o[2*j+1]=pk(f[0],f[1]);
      if constexpr(j&1) st4<(j>>1)*2048>((u32x4){o[2*j-2],o[2*j-1],o[2*j],o[2*j+1]},uv,wssrd,us);
      if constexpr(j==1) __builtin_amdgcn_sched_barrier(0);
    });
    sfor<HW,HW+ND>([&](auto ic){ LW2(decltype(ic)::v); });
    waitv<4*ND>();
    flag(80+(xcd&1)*88+n1,false);
    sfor<HW+ND,RB>([&](auto ic){ LW2(decltype(ic)::v); });
  } else {
    if(bb<2){
      const int hid=r*8+xcd;
      u32x2 ep; epoch(ep);
      unsigned tp1[PF1+1], tp3[PF3+1];
      const int f0=(fq*22)>>2; const unsigned span=(fq==3?11:12)*64-4;
      auto T1=[&](int n){ asm volatile("buffer_load_dword %0, %1, %2, %3 offen" : "=&v"(tp1[n]) : "v"(row*(F*2)+min((lane&7)*128u,span)),"s"(w1srd),"s"((64+n*32+w*8+r)*(8*F*2)+f0*128) : "memory"); };
      #pragma unroll
      for(int n=0;n<PF1;n++) T1(n);
      asm volatile("s_waitcnt vmcnt(%8)" : "+v"(xr[0]),"+v"(xr[1]),"+v"(xr[2]),"+v"(xr[3]),"+v"(xr[4]),"+v"(xr[5]),"+v"(xr[6]),"+v"(xr[7]) : "i"(PF1) : "memory");
      epoch_w(ep);
      constexpr int PITCH=1024+64;
      #pragma unroll
      for(int rr=0;rr<2;rr++){
        float s0=0,s1=0;
        #pragma unroll
        for(int j=0;j<8;j++){ s0=dot2(xr[rr*4+(j>>1)][(j&1)*2],0x3f803f80u,s0); s1=dot2(xr[rr*4+(j>>1)][(j&1)*2+1],0x3f803f80u,s1); }
        float nm=rsum(s0+s1)*(-1.f/D);
        f32x2 d[16], y0={0,0}, y1={0,0}; float e[2][16]; center16(e[0],xr[rr*4],xr[rr*4+1],nm); center16(e[1],xr[rr*4+2],xr[rr*4+3],nm);
        #pragma unroll
        for(int j=0;j<16;j++){ d[j]=(f32x2){e[j>>3][(2*j)&15],e[j>>3][(2*j+1)&15]}; if(j&1) y1+=d[j]*d[j]; else y0+=d[j]*d[j]; }
        y0+=y1;
        float rs=__builtin_amdgcn_rsqf(rsum(y0[0]+y0[1])*(1.f/D)+1e-5f);
        #pragma unroll
        for(int c=0;c<4;c++){ u32x4 o;
          #pragma unroll
          for(int j=0;j<4;j++){ f32x2 t=d[4*c+j]*rs; o[j]=pk(t[0],t[1]); }
          *(LDS u32x4*)(sm+(w*8+rr*4+g)*PITCH+c*256+i*16)=o; }
      }
      bar();
      LDS char* tp=sm+(8*hh+q)*PITCH+w*256+cb*32+(p&1)*16+(p>>1)*8;
      u32x4 hv[8];
      #pragma unroll
      for(int c=0;c<8;c++){
        LDS char* ad=tp+(c>>2)*128+((c>>1)&1)*16*PITCH+(c&1)*64;
        u32x2 lo=trd(ad), hi=trd(ad+4*PITCH); hv[c]=(u32x4){lo[0],lo[1],hi[0],hi[1]};
      }
      __builtin_amdgcn_sched_barrier(0);
      #pragma unroll
      for(int c=0;c<8;c++) asm volatile("buffer_store_dwordx4 %0, %1, %2, %3 offen offset:%4 sc1\n\ts_nop 1" :: "v"(hv[c]),"v"(l16),"s"(wssrd),"s"(WS_H+(hid*8+w*2+(c>>2))*4096u),"i"((c&3)*1024) : "memory");
      waitv<0>();
      flag(hid,true);
      {
        #pragma unroll
        for(int n=0;n<PF3;n++) asm volatile("buffer_load_dword %0, %1, %2, %3 offen" : "=&v"(tp3[n]) : "v"(row*(F*2)+min((lane&7)*128u,span)),"s"(mk_srd(w1,S*F*2)),"s"((64+PF1*32+PFS*8+n*32+w*8+r)*(8*F*2)+f0*128) : "memory"); }
      {
        u32x2 pt; const unsigned pv=lane<4?lane*2048u:OOB, pi=(4*r+w)>>1;
        poll_issue(pt,pv,pi); asm volatile("s_waitcnt vmcnt(0)" : "+v"(pt) :: "memory"); poll_spin(pt,pv,pi);
        #pragma unroll
        for(int n=0;n<PF3;n++) asm volatile("" :: "v"(tp3[n]));
        #pragma unroll
        for(int n=0;n<PF1;n++) asm volatile("" :: "v"(tp1[n]));
        unsigned t0,t1,t2,t3; const unsigned so=WS_H+(pi*8+(xcd&1)*4)*4096u+(w&1)*8192u, o1=16*32768, o2=32*32768;
        asm volatile("buffer_load_dword %0, %4, %5, %6 offen sc1\n\tbuffer_load_dword %1, %4, %5, %7 offen sc1\n\tbuffer_load_dword %2, %4, %5, %8 offen sc1\n\tbuffer_load_dword %3, %4, %5, %9 offen sc1\n\ts_waitcnt vmcnt(0)"
          : "=&v"(t0),"=&v"(t1),"=&v"(t2),"=&v"(t3) : "v"(lane*128),"s"(wssrd),"s"(so),"s"(so+o1),"s"(so+o2),"s"(so+o1+o2) : "memory");
      }
      {
        unsigned t0;
        _Pragma("nounroll") for(int n=P2S+w*8+r;n<172;n+=32) asm volatile("buffer_load_dword %0, %1, %2, %3 offen" : "=&v"(t0) : "v"((lane&7)<2?OOB:row*4096+(lane&7)*128),"s"(w2srd),"s"(((xcd&1)?171-n:n)*32768+fq*1024) : "memory");
        waitv<0>();
      }
    } else {
      u32x2 ep; epoch(ep);
      { unsigned t0; const int f0=(fq*22)>>2; const unsigned span=(fq==3?11:12)*64-4;
        _Pragma("nounroll") for(int n=PF1*32+w*2+(r&1);n<PF1*32+PFS*8;n+=8) asm volatile("buffer_load_dword %0, %1, %2, %3 offen" : "=&v"(t0) : "v"(row*(F*2)+min((lane&7)*128u,span)),"s"(w1srd),"s"((64+n)*(8*F*2)+f0*128) : "memory");
        waitv<0>(); }
      asm volatile("s_waitcnt vmcnt(0)" :: "v"(tc) : "memory");
      epoch_w(ep); flag(bid,true);
      { unsigned t0;
        _Pragma("nounroll") for(int n=w*2+(r&1);n<P2S;n+=8) asm volatile("buffer_load_dword %0, %1, %2, %3 offen" : "=&v"(t0) : "v"((lane&7)<2?OOB:row*4096+(lane&7)*128),"s"(w2srd),"s"(((xcd&1)?171-n:n)*32768+fq*1024) : "memory");
        waitv<0>(); }
    }
    ldb2();
    asm volatile("s_waitcnt vmcnt(0)" : "+v"(bias2) :: "memory");
    sfor<0,RB>([&](auto ic){ LW2(decltype(ic)::v); });
  }
  {
    asm volatile("" : "+s"(rot2),"+s"(ring),"+s"(s128));
    const unsigned pv=lane<6?lane*512u:OOB, pi=80+(xcd&1)*88+(k2>>1)*4+a4; u32x2 pt;
    R4 U[11]; f32x16 acc[4];
    auto LU=[&](int t){ bool o=t>=n2; ldf(U[t],o?OOB:av0,o?OOB:av1,wssrd,WS_U+(kb2(t)*8+a)*4096u); };
    sfor<0,11>([&](auto ic){ constexpr int t=decltype(ic)::v;
      constexpr int li=t==0?RB-1:t+RB-2, last=li<10?li:10;
      waitv<4*(last-t)+(t>PI2&&t<=PB2&&t<=PI2+RB-1?1:0)+(t>PB2?4*HV:0)>();
      rdw<16*t,((t+2)%RB)*4096,1024>(q0,q1);
      if constexpr(t>0&&t+RB-1<11){ asm volatile("s_waitcnt lgkmcnt(8)" ::: "memory"); LW2(t+RB-1); }
      if constexpr(t==PI2) poll_issue(pt,pv,pi);
      if constexpr(t==PB2){
        constexpr int after=PI2+RB-1<10?10-(PI2+RB-1):0;
        wait2<4*after>(pt);
        poll_spin(pt,pv,pi);
        sfor<0,HV>([&](auto jc){ LU(decltype(jc)::v); });
        #pragma unroll
        for(int j=0;j<16;j++) acc[0][j]=flo(bias2);
        asm volatile("" : "+v"(acc[0]));
      }
    });
    RA ra; red_adr(ra,red,w,l16);
    unsigned ov=(lane&31)*(D*2)+hh*16, os=(stile*64+(w>>1)*32)*(D*2)+a*128+(w&1)*64; i32x4 osrd=mk_srd(out,S*D*2);
    unsigned bv=bid!=79?OOB:threadIdx.x*128u+(threadIdx.x>=80&&threadIdx.x<168?0u:32768u); u32x2 pb;
    asm volatile("" : "+s"(slb)); slb=bid==79?slb-32768:slb;
    asm volatile("" : "+v"(ov),"+s"(os),"+s"(osrd),"+v"(bv),"+s"(slb));
    waitl();
    sfor<0,11>([&](auto ic){ constexpr int t=decltype(ic)::v;
      constexpr int last=t+HV-1<10?t+HV-1:10;
      wait1<4*(last-t)>(U[t]);
      if constexpr(t+HV<11){ const bool o=t+HV>=n2; mml<16*t,t==0,false>(acc,U[t],U[t+HV],o?OOB:av0,o?OOB:av1,wssrd,WS_U+(kb2(t+HV)*8+a)*4096u); }
      else mm<16*t,false>(acc,U[t]);
    });
    poll_issue(pb,bv,0);
    red_put(acc,ra);
    P12 d; red_ld(d,ra.p[0]);
    #pragma unroll
    for(int k=0;k<2;k++){ f32x4 v0=red_get(acc[0],2*k,d), v1=red_get(acc[0],2*k+1,d);
      asm volatile("buffer_store_dwordx4 %0, %1, %2, %3 offen offset:%4 \n\ts_nop 1" :: "v"((u32x4){pk(v0[0],v0[1]),pk(v0[2],v0[3]),pk(v1[0],v1[1]),pk(v1[2],v1[3])}),"v"(ov),"s"(osrd),"s"(os),"i"(k*32) : "memory"); }
    wait2<2>(pb);
    if(bid==79){
      poll_spin(pb,bv,0);
      asm volatile("s_barrier\n\tbuffer_store_dword %0, %2, %1, 0 offen sc1" :: "v"(tlo),"s"(wssrd),"v"(threadIdx.x==0?0u:OOB) : "memory");
    }
  }
}

extern "C" void kernel_launch(void* const* d_in, const int* in_sizes, int n_in,
                              void* d_out, int out_size, void* d_ws, size_t ws_size,
                              hipStream_t stream) {
  (void)hipFuncSetAttribute((const void*)mixer,hipFuncAttributeMaxDynamicSharedMemorySize,LDSB);
  mixer<<<256,256,LDSB,stream>>>((const u16*)d_in[0],(const u16*)d_in[1],(const u16*)d_in[2],(const u16*)d_in[3],(const u16*)d_in[4],(u16*)d_out,(char*)d_ws);
}
```

```cpp
#include <hip/hip_runtime.h>
#include <stdint.h>
typedef unsigned short u16;
typedef __attribute__((ext_vector_type(2))) __bf16 bf16x2;
typedef __attribute__((ext_vector_type(4))) __bf16 bf16x4;
typedef __attribute__((ext_vector_type(2))) float f32x2;
typedef __attribute__((ext_vector_type(4))) float f32x4;
typedef __attribute__((ext_vector_type(16))) float f32x16;
typedef __attribute__((ext_vector_type(4))) unsigned u32x4;
typedef __attribute__((ext_vector_type(2))) unsigned u32x2;
typedef __attribute__((ext_vector_type(4))) int i32x4;
typedef __attribute__((ext_vector_type(8))) unsigned u32x8;
#define LDS __attribute__((address_space(3)))
#define DEV __device__ __forceinline__
constexpr int S=2048, D=512, F=1376;
constexpr int HW=3, HV=3;
constexpr int PB=13;
constexpr int PI2=2, PB2=5;
constexpr int ND=2;
constexpr int PF1=2, PFS=8, PF3=2, P2S=44;
#define APOL "sc1"
constexpr unsigned WS_SL=4096, WS_H=WS_SL+8*32768, WS_U=WS_H+64*32*1024, WS_END=WS_U+43*32*1024;
constexpr unsigned OOB=0x7ff00000u;
constexpr int RB=7, RING=RB*4096, L_RED=4*RING, LDSB=L_RED+49152;

DEV i32x4 mk_srd(const void* p, unsigned bytes){
  i32x4 r; uint64_t a=(uint64_t)p;
  r[0]=(int)(unsigned)a; r[1]=(int)(unsigned)(a>>32); r[2]=(int)bytes; r[3]=0x00020000; return r;
}
template<int N> DEV void waitv(){ asm volatile("s_waitcnt vmcnt(%0)" :: "i"(N) : "memory"); }
DEV void waitl(){ asm volatile("s_waitcnt lgkmcnt(0)" ::: "memory"); }
template<int I> struct IC{ static constexpr int v=I; };
template<int B,int E,class Fn> DEV void sfor(Fn&& f){ if constexpr(B<E){ f(IC<B>{}); sfor<B+1,E>(f); } }
DEV unsigned pk(float lo, float hi){ bf16x2 v; v[0]=(__bf16)lo; v[1]=(__bf16)hi; return __builtin_bit_cast(unsigned,v); }
DEV float flo(unsigned v){ return __uint_as_float(v<<16); }
DEV float fhi(unsigned v){ return __uint_as_float(v&0xffff0000u); }
DEV void bar(){ asm volatile("s_waitcnt lgkmcnt(0)\n\ts_barrier" ::: "memory"); }
template<int CTRL> DEV float dppadd(float v){ return v+__builtin_bit_cast(float,__builtin_amdgcn_update_dpp(0,__builtin_bit_cast(int,v),CTRL,0xf,0xf,true)); }
DEV float rsum(float v){ v=dppadd<0xB1>(v); v=dppadd<0x4E>(v); v=dppadd<0x141>(v); return dppadd<0x140>(v); }
DEV float dot2(unsigned a, unsigned b, float c){ return __builtin_amdgcn_fdot2_f32_bf16(__builtin_bit_cast(bf16x2,a),__builtin_bit_cast(bf16x2,b),c,false); }
#define D2(l,h,x) "v_dot2_f32_bf16 " l ", " x ", %24, %26\n\tv_dot2_f32_bf16 " h ", " x ", %25, %26\n\t"
DEV void center16(float (&d)[16], u32x4 a, u32x4 b, float c){
  asm(D2("%0","%1","%16") D2("%2","%3","%17") D2("%4","%5","%18") D2("%6","%7","%19") D2("%8","%9","%20") D2("%10","%11","%21") D2("%12","%13","%22") D2("%14","%15","%23") "s_nop 2"
    : "=&v"(d[0]),"=&v"(d[1]),"=&v"(d[2]),"=&v"(d[3]),"=&v"(d[4]),"=&v"(d[5]),"=&v"(d[6]),"=&v"(d[7]),"=&v"(d[8]),"=&v"(d[9]),"=&v"(d[10]),"=&v"(d[11]),"=&v"(d[12]),"=&v"(d[13]),"=&v"(d[14]),"=&v"(d[15])
    : "v"(a[0]),"v"(a[1]),"v"(a[2]),"v"(a[3]),"v"(b[0]),"v"(b[1]),"v"(b[2]),"v"(b[3]),"s"(0x00003f80u),"s"(0x3f800000u),"v"(c));
}
DEV f32x2 gelu2(f32x2 a){
  f32x2 z=a*(a*a*-0.10294324f-2.30220819f);
  f32x2 e={__builtin_amdgcn_exp2f(z[0]),__builtin_amdgcn_exp2f(z[1])}; e+=1.f;
  return a*(f32x2){__builtin_amdgcn_rcpf(e[0]),__builtin_amdgcn_rcpf(e[1])};
}
DEV u32x2 trd(LDS char* p){ return __builtin_bit_cast(u32x2,__builtin_amdgcn_ds_read_tr16_b64_v4bf16((LDS bf16x4*)p)); }
struct R4{ u32x4 v[4]; };
template<int N> DEV void wait1(R4& a){ asm volatile("s_waitcnt vmcnt(%4)" : "+v"(a.v[0]),"+v"(a.v[1]),"+v"(a.v[2]),"+v"(a.v[3]) : "i"(N) : "memory"); }
template<int N> DEV void wait2(u32x2& a){ asm volatile("s_waitcnt vmcnt(%1)" : "+v"(a) : "i"(N) : "memory"); }
DEV void ldf(R4& d, unsigned v0, unsigned v1, i32x4 srd, unsigned so){
  asm volatile("buffer_load_dwordx4 %0, %4, %6, %7 offen " APOL "\n\tbuffer_load_dwordx4 %1, %5, %6, %7 offen " APOL "\n\t"
               "buffer_load_dwordx4 %2, %4, %6, %7 offen offset:2048 " APOL "\n\tbuffer_load_dwordx4 %3, %5, %6, %7 offen offset:2048 " APOL
    : "=&v"(d.v[0]),"=&v"(d.v[1]),"=&v"(d.v[2]),"=&v"(d.v[3]) : "v"(v0),"v"(v1),"s"(srd),"s"(so) : "memory");
}
DEV void dma4(unsigned v, i32x4 srd, unsigned so, unsigned step, unsigned l){
  unsigned t;
  asm volatile("s_mov_b32 m0, %5\n\ts_add_u32 %0, %3, %4\n\tbuffer_load_dwordx4 %1, %2, %3 offen lds\n\t"
               "s_add_u32 m0, m0, 0x400\n\ts_nop 0\n\tbuffer_load_dwordx4 %1, %2, %0 offen lds\n\t"
               "s_add_u32 m0, m0, 0x400\n\ts_add_u32 %0, %0, %4\n\tbuffer_load_dwordx4 %1, %2, %0 offen lds\n\t"
               "s_add_u32 m0, m0, 0x400\n\ts_add_u32 %0, %0, %4\n\tbuffer_load_dwordx4 %1, %2, %0 offen lds"
    : "=&s"(t) : "v"(v),"s"(srd),"s"(so),"s"(step),"s"(l) : "memory","scc");
}
#define TRA(r,p,o) "ds_read_b64_tr_b16 a[%2+" #r ":%2+" #r "+1], " p " offset:" o "\n\t"
template<int A,int OFF,int T> DEV void rdw(unsigned r0, unsigned r1){
  asm volatile(TRA(0,"%0","%3") TRA(2,"%0","%3+%4") TRA(4,"%1","%3") TRA(6,"%1","%3+%4")
               TRA(8,"%0","%3+2048") TRA(10,"%0","%3+2048+%4") TRA(12,"%1","%3+2048") TRA(14,"%1","%3+2048+%4")
    :: "v"(r0),"v"(r1),"n"(A),"n"(OFF),"n"(T) : "memory");
}
#define MW(c,r,x,s) "v_mfma_f32_32x32x16_bf16 " c ", a[%[A]+" #r ":%[A]+" #r "+3], " x ", " s "\n\t"
#define MX(c,r,x,s) "v_mfma_f32_32x32x16_bf16 " c ", " x ", a[%[A]+" #r ":%[A]+" #r "+3], " s "\n\t"
#define MM(M,z0,z1,z2,z3,l0,l1,l2,l3) M("%0",0,"%[h0]",z0) l0 M("%1",0,"%[h1]",z1) M("%2",4,"%[h0]",z2) l1 M("%3",4,"%[h1]",z3) \
  M("%0",8,"%[h2]","%0") l2 M("%1",8,"%[h3]","%1") M("%2",12,"%[h2]","%2") l3 M("%3",12,"%[h3]","%3")
#define LF(d,v,o) "buffer_load_dwordx4 " d ", " v ", %[srd], %[so] offen " o APOL "\n\t"
#define LFS LF("%4","%[v0]",""),LF("%5","%[v1]",""),LF("%6","%[v0]","offset:2048 "),LF("%7","%[v1]","offset:2048 ")
#define LD0 "s_mov_b32 m0, %[l]\n\ts_mov_b32 %4, %[so]\n\tbuffer_load_dwordx4 %[v0], %[srd], %4 offen lds\n\t"
#define LD1 "s_add_u32 m0, m0, 0x400\n\ts_add_u32 %4, %4, 0x8000\n\tbuffer_load_dwordx4 %[v0], %[srd], %4 offen lds\n\t"
#define LDS4 LD0,LD1,LD1,LD1
#define NOL "","","",""
#define ZC "%0","0","0","0"
#define CZ "+v"(c[0]),"=&v"(c[1]),"=&v"(c[2]),"=&v"(c[3])
#define AC "%0","%1","%2","%3"
#define MMX(...) MM(__VA_ARGS__)
#define CO(m) m(c[0]),m(c[1]),m(c[2]),m(c[3])
#define HI [h0]"v"(h.v[0]),[h1]"v"(h.v[1]),[h2]"v"(h.v[2]),[h3]"v"(h.v[3]),[A]"n"(A)
#define NO "=&v"(n.v[0]),"=&v"(n.v[1]),"=&v"(n.v[2]),"=&v"(n.v[3])
#define LI [v0]"v"(v0),[v1]"v"(v1),[srd]"s"(srd),[so]"s"(so)
template<int A,bool FIRST,bool WROWS> DEV void mml(f32x16 (&c)[4], const R4& h, R4& n, unsigned v0, unsigned v1, i32x4 srd, unsigned so){
  if constexpr(FIRST){
    if constexpr(WROWS) asm volatile(MMX(MW,ZC,LFS) : CZ,NO : HI,LI : "memory"); else asm volatile(MMX(MX,ZC,LFS) : CZ,NO : HI,LI : "memory");
  } else {
    if constexpr(WROWS) asm volatile(MMX(MW,AC,LFS) : CO("+v"),NO : HI,LI : "memory"); else asm volatile(MMX(MX,AC,LFS) : CO("+v"),NO : HI,LI : "memory");
  }
}
template<int A> DEV void mmd(f32x16 (&c)[4], const R4& h, unsigned v0, i32x4 srd, unsigned so, unsigned l){
  unsigned t; asm volatile(MMX(MW,AC,LDS4) : CO("+v"),"=&s"(t) : HI,[v0]"v"(v0),[srd]"s"(srd),[so]"s"(so),[l]"s"(l) : "memory","scc");
}
template<int A,bool WROWS> DEV void mm(f32x16 (&c)[4], const R4& h){
  if constexpr(WROWS) asm volatile(MMX(MW,AC,NOL) : CO("+v") : HI); else asm volatile(MMX(MX,AC,NOL) : CO("+v") : HI);
}
struct RA{ LDS char* p[4]; };
DEV void red_adr(RA& a, LDS char* red, int w, unsigned l16){
  #pragma unroll
  for(int q=0;q<4;q++) a.p[q]=red+(q^w)*12288+(q?q-1:0)*4096+l16;
  asm volatile("" : "+v"(a.p[0]),"+v"(a.p[1]),"+v"(a.p[2]),"+v"(a.p[3]));
}
DEV void red_put(f32x16 (&c)[4], const RA& a){
  asm volatile("s_nop 7\n\ts_nop 7" : "+v"(c[0]),"+v"(c[1]),"+v"(c[2]),"+v"(c[3]));
  #pragma unroll
  for(int n=0;n<12;n++){ const int q=1+n/4, j=n&3; *(LDS f32x4*)(a.p[q]+j*1024)=(f32x4){c[q][4*j],c[q][4*j+1],c[q][4*j+2],c[q][4*j+3]}; }
  bar();
}
template<int O> DEV void st4(u32x4 d, unsigned v, i32x4 srd, unsigned so){
  asm volatile("buffer_store_dwordx4 %0, %1, %2, %3 offen offset:%4 sc1\n\ts_nop 1" :: "v"(d),"v"(v),"s"(srd),"s"(so),"i"(O) : "memory");
}
struct P12{ f32x4 v[12]; };
DEV void red_ld(P12& d, LDS char* p){
  #pragma unroll
  for(int n=0;n<12;n++) d.v[n]=*(LDS f32x4*)(p+(n%3)*4096+(n/3)*1024);
  __builtin_amdgcn_sched_barrier(0);
}
DEV f32x4 red_get(const f32x16& c, int j, const P12& d){ return ((f32x4){c[4*j],c[4*j+1],c[4*j+2],c[4*j+3]}+d.v[3*j])+(d.v[3*j+1]+d.v[3*j+2]); }

__global__ __launch_bounds__(256,1) void mixer(const u16*, const u16*, const u16*, const u16*, const u16*, u16*, char*){
  extern __shared__ __attribute__((aligned(16))) char smem[];
  LDS char* sm=(LDS char*)smem;
  u32x8 k8; u32x4 k4; u32x2 k2a; u32x4 xr[8]; unsigned tc;
  { unsigned long long pc; unsigned t;
    asm volatile("s_load_dwordx8 %0, %6, 0x0\n\ts_load_dwordx4 %1, %6, 0x20\n\ts_load_dwordx2 %2, %6, 0x30\n\t"
                 "s_lshr_b32 %5, %7, 4\n\ts_cmp_lg_u32 %5, 4\n\ts_cbranch_scc1 1f\n\ts_getpc_b64 %4\n\tglobal_load_dword %3, %8, %4\n1:"
      : "=&s"(k8),"=&s"(k4),"=&s"(k2a),"=&v"(tc),"=&s"(pc),"=&s"(t) : "s"(__builtin_amdgcn_kernarg_segment_ptr()),"s"(blockIdx.x),"v"(threadIdx.x*56) : "memory","scc","a255");
    __builtin_amdgcn_sched_barrier(0); }
  const int bid=blockIdx.x, xcd=bid&7, r=bid>>3, a4=r&3, bb=r>>2, fq=xcd>>1, a=(xcd&1)*4+a4;
  const int lane=threadIdx.x&63; const int w=__builtin_amdgcn_readfirstlane(threadIdx.x>>6);
  const int n1=fq*22+r-10, ft=n1>>2, a1=(xcd&1)*4+(n1&3), stile=fq*8+bb;
  const unsigned l16=lane*16, g=lane>>4, i=lane&15, q=i>>2, p=i&3, hh=g>>1, cb=g&1;
  unsigned tlo,thi;
  auto epoch_w=[&](u32x2& ep){ asm volatile("s_waitcnt lgkmcnt(0)" : "+s"(ep) :: "memory"); tlo=ep[0]+1u; thi=ep[1]; };
  const unsigned row=lane>>3, ch=(lane&7)^(4*((row>>1)&1));
  unsigned ring=(unsigned)(size_t)sm+w*RING, s128=stile*128, slb=WS_SL+xcd*32768;
  const unsigned av0=l16+(w&1)*1024, av1=av0^1024, sw64=(((q>>1)^(w>>1))&1)*64;
  const int n2=w<3?11:10, k2=11*w; int rot2=(bb*11>>3)+a4*3; if(rot2>=n2) rot2-=n2;
  const int rot1=n1+((r-10)>>2);
  auto kb=[&](int t){ return 4*((t&12)|((t+rot1)&3))+w; };
  unsigned wv=row*(F*2)+(ft==21?(ch&3):ch)*16, xv=w*8192+g*1024+i*16, xo=bid*32768u, xo2=xo+4096, w0=kb(0)*(32*F*2)+ft*128;
  asm volatile("s_waitcnt lgkmcnt(0)" : "+s"(k8),"+s"(k4),"+s"(k2a),"+s"(ring),"+v"(wv),"+v"(xv),"+s"(xo),"+s"(xo2),"+s"(w0) :: "memory");
  auto ptr=[](unsigned lo, unsigned hi){ return (char*)(((unsigned long long)hi<<32)|lo); };
  const char *x=ptr(k8[0],k8[1]), *w1=ptr(k8[2],k8[3]), *b1=ptr(k8[4],k8[5]), *w2=ptr(k8[6],k8[7]), *b2=ptr(k4[0],k4[1]); char *out=ptr(k4[2],k4[3]), *ws=ptr(k2a[0],k2a[1]);
  const i32x4 w1srd=mk_srd(w1,S*F*2);
  { unsigned t;
    asm volatile("s_cmp_lt_u32 %11, 0x280000\n\ts_cbranch_scc1 1f\n\t"
                 "s_mov_b32 m0, %16\n\ts_add_u32 %8, %15, %17\n\tbuffer_load_dwordx4 %13, %14, %15 offen lds\n\t"
                 "s_add_u32 m0, m0, 0x400\n\ts_nop 0\n\tbuffer_load_dwordx4 %13, %14, %8 offen lds\n\t"
                 "s_add_u32 m0, m0, 0x400\n\ts_add_u32 %8, %8, %17\n\tbuffer_load_dwordx4 %13, %14, %8 offen lds\n\t"
                 "s_add_u32 m0, m0, 0x400\n\ts_add_u32 %8, %8, %17\n\tbuffer_load_dwordx4 %13, %14, %8 offen lds\n\ts_branch 2f\n"
                 "1:\n\ts_cmp_gt_u32 %11, 0x1fffff\n\ts_cbranch_scc1 2f\n\t"
                 "buffer_load_dwordx4 %0, %9, %10, %11 offen\n\tbuffer_load_dwordx4 %1, %9, %10, %11 offen offset:256\n\t"
                 "buffer_load_dwordx4 %2, %9, %10, %11 offen offset:512\n\tbuffer_load_dwordx4 %3, %9, %10, %11 offen offset:768\n\ts_barrier\n\t"
                 "buffer_load_dwordx4 %4, %9, %10, %12 offen\n\tbuffer_load_dwordx4 %5, %9, %10, %12 offen offset:256\n\t"
                 "buffer_load_dwordx4 %6, %9, %10, %12 offen offset:512\n\tbuffer_load_dwordx4 %7, %9, %10, %12 offen offset:768\n\ts_barrier\n2:"
      : "=&v"(xr[0]),"=&v"(xr[1]),"=&v"(xr[2]),"=&v"(xr[3]),"=&v"(xr[4]),"=&v"(xr[5]),"=&v"(xr[6]),"=&v"(xr[7]),"=&s"(t)
      : "v"(xv),"s"(mk_srd(x,S*D*2)),"s"(xo),"s"(xo2),"v"(wv),"s"(w1srd),"s"(w0),"s"(ring),"i"(8*F*2) : "memory","scc");
    __builtin_amdgcn_sched_barrier(0); }
  asm volatile("" : "+s"(s128),"+s"(rot2),"+s"(slb));
  const i32x4 wssrd=mk_srd(ws,WS_END), w2srd=mk_srd(w2,S*F*2);
  auto epoch=[&](u32x2& ep){ asm volatile("s_load_dwordx2 %0, %1, 0x0" : "=&s"(ep) : "s"(ws) : "memory"); };
  auto kb2=[&](int t){ int k=t+rot2; if(k>=n2) k-=n2; return k2+k; };
  auto LW2=[&](int t){ dma4(row*(S*2)+ch*16,w2srd,kb2(t)*(32*S*2)+s128,8*S*2,ring+((t+2)%RB)*4096); };
  auto flag=[&](unsigned line,bool all){ u32x2 t={tlo,thi}; const unsigned tid=threadIdx.x;
    asm volatile("s_barrier\n\tbuffer_store_dwordx2 %0, %1, %2, %3 offen sc1" :: "v"(t),"v"(all?(tid<8?tid*32768u:OOB):(tid<4?(tid*2+(xcd&1))*32768u:OOB)),"s"(wssrd),"s"(WS_SL+line*128u) : "memory"); };
  auto poll_issue=[&](u32x2& t,unsigned v,unsigned idx){ asm volatile("buffer_load_dwordx2 %0, %1, %2, %3 offen sc1" : "=&v"(t) : "v"(v),"s"(wssrd),"s"(slb+idx*128u) : "memory"); };
  auto poll_spin=[&](u32x2& t,unsigned v,unsigned idx){
    _Pragma("nounroll") for(int spin=0;spin<(1<<20);spin++){
      if(__all(v==OOB||(t[0]==tlo && t[1]==thi))) break;
      __builtin_amdgcn_s_sleep(2);
      asm volatile("buffer_load_dwordx2 %0, %1, %2, %3 offen sc1\n\ts_waitcnt vmcnt(0)" : "=&v"(t) : "v"(v),"s"(wssrd),"s"(slb+idx*128u) : "memory");
    } };
  const unsigned q0=ring+(4*hh+q)*128+32*cb+8*p+sw64, q1=q0^64;
  LDS char* red=sm+L_RED;
  unsigned bias2;
  auto ldb2=[&](){ asm volatile("buffer_load_ushort %0, %1, %2, %3 offen" : "=&v"(bias2) : "v"((lane&31)*2),"s"(mk_srd(b2,S*2)),"s"(stile*128+(w>>1)*64) : "memory"); };
    auto LW1=[&](int t){ dma4(wv,w1srd,kb(t)*(32*F*2)+ft*128,8*F*2,ring+(t%RB)*4096); };
  int r2=r; asm volatile("" : "+s"(r2));
  if(r2>=10){
    const int ub=2*ft+(w>>1); unsigned uv=ub<43?av0:OOB, us=WS_U+((ub<43?ub:0)*8+a1)*4096u;
    u32x2 ep; epoch(ep);
    u32x2 B1[4];
    asm volatile("buffer_load_dwordx2 %0, %4, %5, 0 offen\n\tbuffer_load_dwordx2 %1, %4, %5, 0 offen offset:16\n\tbuffer_load_dwordx2 %2, %4, %5, 0 offen offset:32\n\tbuffer_load_dwordx2 %3, %4, %5, 0 offen offset:48"
      : "=&v"(B1[0]),"=&v"(B1[1]),"=&v"(B1[2]),"=&v"(B1[3]) : "v"(ub*64+hh*8),"s"(mk_srd(b1,F*2)) : "memory");
    ldb2();
    sfor<1,RB>([&](auto ic){ LW1(decltype(ic)::v); });
    const unsigned r0=ring+(8*hh+q)*128+32*cb+8*p+sw64, r1=r0^64;
    const unsigned pv=lane<16?lane*512u:OOB, pi=w; u32x2 pt;
    R4 H[16]; f32x16 acc[4];
    auto LH=[&](int t){ ldf(H[t],av0,av1,wssrd,WS_H+(kb(t)*8+a1)*4096u); };
    sfor<0,16>([&](auto ic){ constexpr int t=decltype(ic)::v;
      constexpr int li=t==0?RB-1:t+RB-2, last=li<15?li:15;
      waitv<4*(last-t)+(t<1?5:0)+(t<=PB&&last>=PB?1:0)+(t>PB?4*HW:0)>();
      rdw<16*t,(t%RB)*4096,512>(r0,r1);
      if constexpr(t>0&&t+RB-1<16){ asm volatile("s_waitcnt lgkmcnt(8)" ::: "memory"); LW1(t+RB-1); if constexpr(t+RB-1==PB) poll_issue(pt,pv,pi); }
      if constexpr(t==PB){
        asm volatile("s_waitcnt vmcnt(%6)" : "+v"(B1[0]),"+v"(B1[1]),"+v"(B1[2]),"+v"(B1[3]),"+v"(bias2),"+v"(pt) : "i"(4*(15-PB)) : "memory");
        epoch_w(ep);
        poll_spin(pt,pv,pi);
        sfor<0,HW>([&](auto jc){ LH(decltype(jc)::v); });
        #pragma unroll
        for(int j=0;j<8;j++){ acc[0][2*j]=flo(B1[j>>1][j&1]); acc[0][2*j+1]=fhi(B1[j>>1][j&1]); }
        asm volatile("" : "+v"(acc[0]));
      }
    });
    RA ra; red_adr(ra,red,w,l16); asm volatile("" : "+v"(uv),"+s"(us));
    waitl();
    sfor<0,16>([&](auto ic){ constexpr int t=decltype(ic)::v;
      wait1<4*(HW-1)>(H[t]);
      if constexpr(t+HW<16) mml<16*t,t==0,true>(acc,H[t],H[t+HW],av0,av1,wssrd,WS_H+(kb(t+HW)*8+a1)*4096u);
      else mmd<16*t>(acc,H[t],row*(S*2)+ch*16,w2srd,kb2(t+HW-16)*(32*S*2)+s128,ring+((t+HW-14)%RB)*4096);
    });
    red_put(acc,ra);
    P12 d; red_ld(d,ra.p[0]);
    unsigned o[8];
    sfor<0,4>([&](auto ic){ constexpr int j=decltype(ic)::v;
      f32x4 v=red_get(acc[0],j,d);
      f32x2 e=gelu2((f32x2){v[0],v[1]}), f=gelu2((f32x2){v[2],v[3]});
      o[2*j]=pk(e[0],e[1]); o[2*j+1]=pk(f[0],f[1]);
      if constexpr(j&1) st4<(j>>1)*2048>((u32x4){o[2*j-2],o[2*j-1],o[2*j],o[2*j+1]},uv,wssrd,us);
      if constexpr(j==1) __builtin_amdgcn_sched_barrier(0);
    });
    sfor<HW,HW+ND>([&](auto ic){ LW2(decltype(ic)::v); });
    waitv<4*ND>();
    flag(80+(xcd&1)*88+n1,false);
    sfor<HW+ND,RB>([&](auto ic){ LW2(decltype(ic)::v); });
  } else {
    if(bb<2){
      const int hid=r*8+xcd;
      u32x2 ep; epoch(ep);
      unsigned th[4], tp1[PF1+1], tp3[PF3+1];
      const int f0=(fq*22)>>2; const unsigned span=(fq==3?11:12)*64-4;
      auto T1=[&](int n){ asm volatile("buffer_load_dword %0, %1, %2, %3 offen" : "=&v"(tp1[n]) : "v"(row*(F*2)+min((lane&7)*128u,span)),"s"(w1srd),"s"((64+n*32+w*8+r)*(8*F*2)+f0*128) : "memory"); };
      epoch_w(ep);
      constexpr int PITCH=1024+64;
      #pragma unroll
      for(int rr=0;rr<2;rr++){
        asm volatile("s_waitcnt vmcnt(%4)" : "+v"(xr[rr*4]),"+v"(xr[rr*4+1]),"+v"(xr[rr*4+2]),"+v"(xr[rr*4+3]) : "i"(rr?PF1:4) : "memory");
        #pragma unroll
        for(int n=0;n<(rr?0:PF1);n++) T1(n);
        float s0=0,s1=0;
        #pragma unroll
        for(int j=0;j<8;j++){ s0=dot2(xr[rr*4+(j>>1)][(j&1)*2],0x3f803f80u,s0); s1=dot2(xr[rr*4+(j>>1)][(j&1)*2+1],0x3f803f80u,s1); }
        float nm=rsum(s0+s1)*(-1.f/D);
        f32x2 d[16], y0={0,0}, y1={0,0}; float e[2][16]; center16(e[0],xr[rr*4],xr[rr*4+1],nm); center16(e[1],xr[rr*4+2],xr[rr*4+3],nm);
        #pragma unroll
        for(int j=0;j<16;j++){ d[j]=(f32x2){e[j>>3][(2*j)&15],e[j>>3][(2*j+1)&15]}; if(j&1) y1+=d[j]*d[j]; else y0+=d[j]*d[j]; }
        y0+=y1;
        float rs=__builtin_amdgcn_rsqf(rsum(y0[0]+y0[1])*(1.f/D)+1e-5f);
        #pragma unroll
        for(int c=0;c<4;c++){ u32x4 o;
          #pragma unroll
          for(int j=0;j<4;j++){ f32x2 t=d[4*c+j]*rs; o[j]=pk(t[0],t[1]); }
          *(LDS u32x4*)(sm+(w*8+rr*4+g)*PITCH+c*256+i*16)=o; }
      }
      bar();
      LDS char* tp=sm+(8*hh+q)*PITCH+w*256+cb*32+(p&1)*16+(p>>1)*8;
      u32x4 hv[8];
      #pragma unroll
      for(int c=0;c<8;c++){
        LDS char* ad=tp+(c>>2)*128+((c>>1)&1)*16*PITCH+(c&1)*64;
        u32x2 lo=trd(ad), hi=trd(ad+4*PITCH); hv[c]=(u32x4){lo[0],lo[1],hi[0],hi[1]};
      }
      __builtin_amdgcn_sched_barrier(0);
      #pragma unroll
      for(int c=0;c<8;c++) asm volatile("buffer_store_dwordx4 %0, %1, %2, %3 offen offset:%4 sc1\n\ts_nop 1" :: "v"(hv[c]),"v"(l16),"s"(wssrd),"s"(WS_H+(hid*8+w*2+(c>>2))*4096u),"i"((c&3)*1024) : "memory");
      waitv<0>();
      flag(hid,true);
      {
        #pragma unroll
        for(int n=0;n<PF3;n++) asm volatile("buffer_load_dword %0, %1, %2, %3 offen" : "=&v"(tp3[n]) : "v"(row*(F*2)+min((lane&7)*128u,span)),"s"(mk_srd(w1,S*F*2)),"s"((64+PF1*32+PFS*8+n*32+w*8+r)*(8*F*2)+f0*128) : "memory"); }
      {
        u32x2 pt; const unsigned pv=lane<4?lane*2048u:OOB, pi=(4*r+w)>>1;
        poll_issue(pt,pv,pi); asm volatile("s_waitcnt vmcnt(0)" : "+v"(pt) :: "memory"); poll_spin(pt,pv,pi);
        #pragma unroll
        for(int n=0;n<PF3;n++) asm volatile("" :: "v"(tp3[n]));
        #pragma unroll
        for(int n=0;n<PF1;n++) asm volatile("" :: "v"(tp1[n]));
        const unsigned so=WS_H+(pi*8+(xcd&1)*4)*4096u+(w&1)*8192u, o1=16*32768, o2=32*32768;
        asm volatile("buffer_load_dword %0, %4, %5, %6 offen sc1\n\tbuffer_load_dword %1, %4, %5, %7 offen sc1\n\tbuffer_load_dword %2, %4, %5, %8 offen sc1\n\tbuffer_load_dword %3, %4, %5, %9 offen sc1"
          : "=&v"(th[0]),"=&v"(th[1]),"=&v"(th[2]),"=&v"(th[3]) : "v"(lane*128),"s"(wssrd),"s"(so),"s"(so+o1),"s"(so+o2),"s"(so+o1+o2) : "memory");
      }
      {
        unsigned t0;
        _Pragma("nounroll") for(int n=P2S+w*8+r;n<172;n+=32) asm volatile("buffer_load_dword %0, %1, %2, %3 offen" : "=&v"(t0) : "v"((lane&7)<2?OOB:row*4096+(lane&7)*128),"s"(w2srd),"s"(((xcd&1)?171-n:n)*32768+fq*1024) : "memory");
        asm volatile("s_waitcnt vmcnt(0)" :: "v"(th[0]),"v"(th[1]),"v"(th[2]),"v"(th[3]) : "memory");
      }
    } else {
      u32x2 ep; epoch(ep);
      { unsigned t0; const int f0=(fq*22)>>2; const unsigned span=(fq==3?11:12)*64-4;
        _Pragma("nounroll") for(int n=PF1*32+w*2+(r&1);n<PF1*32+PFS*8;n+=8) asm volatile("buffer_load_dword %0, %1, %2, %3 offen" : "=&v"(t0) : "v"(row*(F*2)+min((lane&7)*128u,span)),"s"(w1srd),"s"((64+n)*(8*F*2)+f0*128) : "memory");
        waitv<0>(); }
      asm volatile("s_waitcnt vmcnt(0)" :: "v"(tc) : "memory");
      epoch_w(ep); flag(bid,true);
      { unsigned t0;
        _Pragma("nounroll") for(int n=w*2+(r&1);n<P2S;n+=8) asm volatile("buffer_load_dword %0, %1, %2, %3 offen" : "=&v"(t0) : "v"((lane&7)<2?OOB:row*4096+(lane&7)*128),"s"(w2srd),"s"(((xcd&1)?171-n:n)*32768+fq*1024) : "memory");
        waitv<0>(); }
    }
    ldb2();
    asm volatile("s_waitcnt vmcnt(0)" : "+v"(bias2) :: "memory");
    sfor<0,RB>([&](auto ic){ LW2(decltype(ic)::v); });
  }
  {
    asm volatile("" : "+s"(rot2),"+s"(ring),"+s"(s128));
    const unsigned pv=lane<6?lane*512u:OOB, pi=80+(xcd&1)*88+(k2>>1)*4+a4; u32x2 pt;
    R4 U[11]; f32x16 acc[4];
    auto LU=[&](int t){ bool o=t>=n2; ldf(U[t],o?OOB:av0,o?OOB:av1,wssrd,WS_U+(kb2(t)*8+a)*4096u); };
    sfor<0,11>([&](auto ic){ constexpr int t=decltype(ic)::v;
      constexpr int li=t==0?RB-1:t+RB-2, last=li<10?li:10;
      waitv<4*(last-t)+(t>PI2&&t<=PB2&&t<=PI2+RB-1?1:0)+(t>PB2?4*HV:0)>();
      rdw<16*t,((t+2)%RB)*4096,1024>(q0,q1);
      if constexpr(t>0&&t+RB-1<11){ asm volatile("s_waitcnt lgkmcnt(8)" ::: "memory"); LW2(t+RB-1); }
      if constexpr(t==PI2) poll_issue(pt,pv,pi);
      if constexpr(t==PB2){
        constexpr int after=PI2+RB-1<10?10-(PI2+RB-1):0;
        wait2<4*after>(pt);
        poll_spin(pt,pv,pi);
        sfor<0,HV>([&](auto jc){ LU(decltype(jc)::v); });
        #pragma unroll
        for(int j=0;j<16;j++) acc[0][j]=flo(bias2);
        asm volatile("" : "+v"(acc[0]));
      }
    });
    RA ra; red_adr(ra,red,w,l16);
    unsigned ov=(lane&31)*(D*2)+hh*16, os=(stile*64+(w>>1)*32)*(D*2)+a*128+(w&1)*64; i32x4 osrd=mk_srd(out,S*D*2);
    unsigned bv=bid!=79?OOB:threadIdx.x*128u+(threadIdx.x>=80&&threadIdx.x<168?0u:32768u); u32x2 pb;
    asm volatile("" : "+s"(slb)); slb=bid==79?slb-32768:slb;
    asm volatile("" : "+v"(ov),"+s"(os),"+s"(osrd),"+v"(bv),"+s"(slb));
    waitl();
    sfor<0,11>([&](auto ic){ constexpr int t=decltype(ic)::v;
      constexpr int last=t+HV-1<10?t+HV-1:10;
      wait1<4*(last-t)>(U[t]);
      if constexpr(t+HV<11){ const bool o=t+HV>=n2; mml<16*t,t==0,false>(acc,U[t],U[t+HV],o?OOB:av0,o?OOB:av1,wssrd,WS_U+(kb2(t+HV)*8+a)*4096u); }
      else mm<16*t,false>(acc,U[t]);
    });
    poll_issue(pb,bv,0);
    red_put(acc,ra);
    P12 d; red_ld(d,ra.p[0]);
    #pragma unroll
    for(int k=0;k<2;k++){ f32x4 v0=red_get(acc[0],2*k,d), v1=red_get(acc[0],2*k+1,d);
      asm volatile("buffer_store_dwordx4 %0, %1, %2, %3 offen offset:%4 \n\ts_nop 1" :: "v"((u32x4){pk(v0[0],v0[1]),pk(v0[2],v0[3]),pk(v1[0],v1[1]),pk(v1[2],v1[3])}),"v"(ov),"s"(osrd),"s"(os),"i"(k*32) : "memory"); }
    wait2<2>(pb);
    if(bid==79){
      poll_spin(pb,bv,0);
      asm volatile("s_barrier\n\tbuffer_store_dword %0, %2, %1, 0 offen sc1" :: "v"(tlo),"s"(wssrd),"v"(threadIdx.x==0?0u:OOB) : "memory");
    }
  }
}

extern "C" void kernel_launch(void* const* d_in, const int* in_sizes, int n_in,
                              void* d_out, int out_size, void* d_ws, size_t ws_size,
                              hipStream_t stream) {
  (void)hipFuncSetAttribute((const void*)mixer,hipFuncAttributeMaxDynamicSharedMemorySize,LDSB);
  mixer<<<256,256,LDSB,stream>>>((const u16*)d_in[0],(const u16*)d_in[1],(const u16*)d_in[2],(const u16*)d_in[3],(const u16*)d_in[4],(u16*)d_out,(char*)d_ws);
}
```
